```python
import torch

def _softmax(x, axis=-1):
    return torch.softmax(x, dim=axis)
def _gelu(x):
    return torch.nn.functional.gelu(x, approximate="tanh")
def _silu(x):
    return torch.nn.functional.silu(x)
def _sigmoid(x):
    return torch.sigmoid(x)
def _layernorm(x, axis=-1, eps=1e-5):
    m = x.mean(dim=axis, keepdim=True)
    v = x.var(dim=axis, keepdim=True, unbiased=False)
    return (x - m) / torch.sqrt(v + eps)
def _scatter_add(tgt, idx, src, axis=0):
    return tgt.clone().index_add_(axis, idx, src)
def _segment_sum(src, seg, n):
    out = torch.zeros((n,) + src.shape[1:], dtype=src.dtype)
    return out.index_add_(0, seg, src)
def _pad(x, pw):
    flat = tuple(v for pair in reversed(pw) for v in pair)
    return torch.nn.functional.pad(x, flat)

kernel_name = 'gated_mix__mha_block_cached__dense_activation_squared_relu__decode'

def setup_inputs():

    _w = (lambda a: (lambda t: t.to(torch.bfloat16) if t.is_floating_point() else t)(torch.as_tensor(a)))
    def _A_setup():
        B, Sq, D = 2, 1, 448
        H, Sk, Dh = 16, 4096, 256
        return {
            "x": _w((lambda *s: torch.randn(*s, dtype=torch.bfloat16) * 0.05)(B, Sq, D)),
            "k_cache": _w((lambda *s: torch.randn(*s, dtype=torch.bfloat16) * 6.4)(B, H, Sk, Dh)),
            "v_cache": _w((lambda *s: torch.randn(*s, dtype=torch.bfloat16) * 3.2)(B, H, Sk, Dh)),
            "wq": _w((lambda *s: torch.randn(*s, dtype=torch.bfloat16) * 6.4)(D, H * Dh) * (D ** -0.5)),
            "wo": _w((lambda *s: torch.randn(*s, dtype=torch.bfloat16) * 3.2)(H * Dh, D) * ((H * Dh) ** -0.5)),
        }
    _A_in = _A_setup()

    def _B_setup():
        B, S, D, F = 2, 1, 448, 1792
        return {
            "x": _w((lambda *s: torch.randn(*s, dtype=torch.bfloat16) * 0.05)(B, S, D)),
            "w1": _w((lambda *s: torch.randn(*s, dtype=torch.bfloat16) * 0.2)(D, F)),
            "b1": _w((lambda *s: torch.randn(*s, dtype=torch.bfloat16) * 0.2)(F)),
            "w2": _w((lambda *s: torch.randn(*s, dtype=torch.bfloat16) * 0.2)(F, D)),
            "b2": _w((lambda *s: torch.randn(*s, dtype=torch.bfloat16) * 0.4)(D)),
        }
    _B_in = _B_setup()

    return {
        "A_x": _A_in["x"],
        "A_k_cache": _A_in["k_cache"],
        "A_v_cache": _A_in["v_cache"],
        "A_wq": _A_in["wq"],
        "A_wo": _A_in["wo"],
        "B_x": _B_in["x"],
        "B_w1": _B_in["w1"],
        "B_b1": _B_in["b1"],
        "B_w2": _B_in["w2"],
        "B_b2": _B_in["b2"],
        "gate_w": _w((lambda *s: torch.randn(*s, dtype=torch.bfloat16) * 0.1)(448) * 16.0),
    }

def reference(A_x, A_k_cache, A_v_cache, A_wq, A_wo, B_x, B_w1, B_b1, B_w2, B_b2, gate_w):
    def _A_ref(x, k_cache, v_cache, wq, wo):
        B, Sq, D = x.shape
        H, Dh = 16, 256
        q = (lambda x, *d: x.permute(*d))((x @ wq).reshape(B, Sq, H, Dh), 0, 2, 1, 3)
        scores = torch.einsum("bhqd,bhkd->bhqk", q, k_cache) * (Dh ** -0.5)
        p = _softmax(scores, axis=-1)
        o = torch.einsum("bhqk,bhkd->bhqd", p, v_cache)
        return (lambda x, *d: x.permute(*d))(o, 0, 2, 1, 3).reshape(B, Sq, H * Dh) @ wo

    def _B_ref(x, w1, b1, w2, b2):
        h = torch.einsum("bsd,df->bsf", x, w1) + b1
        zero = torch.zeros_like(h)
        r = torch.where(h > zero, h, zero)
        h = r * r
        out = torch.einsum("bsf,fd->bsd", h, w2) + b2
        return out

    x = A_x
    g = _sigmoid((x * gate_w).sum(-1, keepdims=True))
    a = _A_ref(x, A_k_cache, A_v_cache, A_wq, A_wo)
    b = _B_ref(x, B_w1, B_b1, B_w2, B_b2)
    return g * a + (1 - g) * b
```

```cpp
#include <hip/hip_runtime.h>
#include <hip/hip_bf16.h>
#include <stdint.h>

typedef unsigned v4u __attribute__((ext_vector_type(4)));
typedef unsigned v2u __attribute__((ext_vector_type(2)));
typedef float f32x4 __attribute__((ext_vector_type(4)));
typedef __bf16 bf16x2 __attribute__((ext_vector_type(2)));
typedef __amdgpu_buffer_rsrc_t rsrc_t;

constexpr int D = 448, H = 16, SK = 4096, DH = 256, F = 1792;
constexpr int NSPLIT = 8, KEYS = SK / NSPLIT;
#define RING 36
#define KV_AUX 2

__device__ __forceinline__ float dot2(unsigned a, unsigned b, float acc) {
  return __builtin_amdgcn_fdot2_f32_bf16(__builtin_bit_cast(bf16x2, a), __builtin_bit_cast(bf16x2, b), acc, false);
}
__device__ __forceinline__ unsigned cvtpk(float lo, float hi) {
  unsigned r; asm("v_cvt_pk_bf16_f32 %0, %1, %2" : "=v"(r) : "v"(lo), "v"(hi)); return r;
}
template <int CTRL> __device__ __forceinline__ float dpp(float x) {
  return __builtin_bit_cast(float, __builtin_amdgcn_mov_dpp(__builtin_bit_cast(int, x), CTRL, 0xf, 0xf, true));
}
constexpr int XOR1 = 0xB1, XOR2 = 0x4E, XOR7 = 0x141, XOR8 = 0x128;
__device__ __forceinline__ float swap16_add(float a, float b) {
  auto s = __builtin_amdgcn_permlane16_swap(__float_as_uint(a), __float_as_uint(b), false, false);
  return __uint_as_float(s[0]) + __uint_as_float(s[1]);
}
__device__ __forceinline__ float swap32_add(float a, float b) {
  auto s = __builtin_amdgcn_permlane32_swap(__float_as_uint(a), __float_as_uint(b), false, false);
  return __uint_as_float(s[0]) + __uint_as_float(s[1]);
}
__device__ __forceinline__ float wave_max(float x) {
  x = fmaxf(x, dpp<XOR1>(x)); x = fmaxf(x, dpp<XOR2>(x)); x = fmaxf(x, dpp<XOR7>(x)); x = fmaxf(x, dpp<XOR8>(x));
  auto s = __builtin_amdgcn_permlane16_swap(__float_as_uint(x), __float_as_uint(x), false, false);
  x = fmaxf(__uint_as_float(s[0]), __uint_as_float(s[1]));
  auto t = __builtin_amdgcn_permlane32_swap(__float_as_uint(x), __float_as_uint(x), false, false);
  return fmaxf(__uint_as_float(t[0]), __uint_as_float(t[1]));
}
__device__ __forceinline__ float wave_sum(float x) {
  x += dpp<XOR1>(x); x += dpp<XOR2>(x); x += dpp<XOR7>(x); x += dpp<XOR8>(x);
  x = swap16_add(x, x);
  return swap32_add(x, x);
}
__device__ __forceinline__ rsrc_t mk(const void* p, unsigned bytes) {
  return __builtin_amdgcn_make_buffer_rsrc(const_cast<void*>(p), 0, (int)bytes, 0x00020000);
}
__device__ __forceinline__ float bflo(unsigned u) { return __uint_as_float(u << 16); }
__device__ __forceinline__ float bfhi(unsigned u) { return __uint_as_float(u & 0xffff0000u); }
__device__ __forceinline__ unsigned perm_lo(unsigned a, unsigned b) { return __builtin_amdgcn_perm(b, a, 0x05040100u); }
__device__ __forceinline__ unsigned perm_hi(unsigned a, unsigned b) { return __builtin_amdgcn_perm(b, a, 0x07060302u); }

constexpr int S_WQ = 0, S_K = 28, S_V = 60, S_WO = 92, S_W2 = 124, S_END = 126;

struct Args {
  const unsigned short *x, *kc, *vc, *wq, *wo, *w1, *b1, *w2, *b2, *gw;
  float* ws; unsigned short* out;
};
constexpr int WS_ML = 0, WS_YA = 512, WS_YF = WS_YA + 256 * D;

__global__ void __launch_bounds__(512, 2) kA(Args a) {
  __shared__ __attribute__((aligned(16))) float lds[16 * 256 + 8 * 64 + 16 + 8 * 16 + 16 + 128];
  float* L_part = lds;
  float* L_p = lds + 16 * 256;
  float* L_ml = L_p + 8 * 64;
  float* L_hp = L_ml + 16;
  float* L_h = L_hp + 8 * 16;
  unsigned* L_q = reinterpret_cast<unsigned*>(L_h + 16);
  __shared__ __attribute__((aligned(16))) float L_y[2 * 8 * D];

  const int bid = blockIdx.x, xcd = bid & 7, idx = bid >> 3;
  const int h = xcd * 2 + (idx >> 4), b = (idx >> 3) & 1, s = idx & 7;
  const int j = h * 8 + s;
  const int tid = threadIdx.x, lane = tid & 63, w = __builtin_amdgcn_readfirstlane(tid >> 6), hh = lane >> 5, c = lane & 31;
  const bool ffn = j < F / 16;

  const rsrc_t r_x = mk(a.x + b * D, D * 2);
  const rsrc_t r_wq = mk(a.wq + h * DH, (unsigned)((D - 1) * H * DH + DH) * 2);
  const size_t kvoff = ((size_t)(b * H + h) * SK + (size_t)s * KEYS) * DH;
  const rsrc_t r_k = mk(a.kc + kvoff, KEYS * DH * 2);
  const rsrc_t r_v = mk(a.vc + kvoff, KEYS * DH * 2);
  const rsrc_t r_wo = mk(a.wo + (size_t)h * DH * D, DH * D * 2);
  const rsrc_t r_w1 = mk(a.w1 + (ffn ? 16 * j : 0), ffn ? (unsigned)((D - 1) * F + 16) * 2 : 0u);
  const rsrc_t r_w2 = mk(a.w2 + (ffn ? (size_t)16 * j * D : 0), ffn ? 16 * D * 2 : 0u);
  const rsrc_t r_b1 = mk(a.b1 + (ffn ? 16 * j : 0), ffn ? 32u : 0u);

  unsigned xp[14];
#pragma unroll
  for (int i = 0; i < 7; ++i) { v2u t = __builtin_amdgcn_raw_buffer_load_b64(r_x, (56 * w + 28 * hh) * 2 + 8 * i, 0, 0); xp[2 * i] = t.x; xp[2 * i + 1] = t.y; }
  const unsigned lane56 = lane < 56 ? 0u : 0x40000000u;
  const unsigned short xl = __builtin_amdgcn_raw_buffer_load_b16(r_x, (56 * w + lane) * 2 + lane56, 0, 0);
  const unsigned short b1v = __builtin_amdgcn_raw_buffer_load_b16(r_b1, (tid - 256) * 2, 0, 0);
  v4u w1a = __builtin_amdgcn_raw_buffer_load_b128(r_w1, (56 * w + lane) * F * 2 + lane56, 0, 0);
  v4u w1b = __builtin_amdgcn_raw_buffer_load_b128(r_w1, (56 * w + lane) * F * 2 + 16 + lane56, 0, 0);

  const unsigned o_wq = (56 * w + 28 * hh) * (H * DH * 2) + 16 * c;
  const unsigned o_kv = w * 32768 + lane * 16;
  const unsigned o_wo = w * 32 * D * 2 + lane * 16 + lane56;
  const unsigned o_w2 = w * 2 * D * 2 + lane * 16 + lane56;
  v4u ring[RING];
#define ISSUE(i_) do { constexpr int q_ = (i_); if constexpr (q_ < S_END) { \
    if constexpr (q_ < S_K) ring[q_ % RING] = __builtin_amdgcn_raw_buffer_load_b128(r_wq, o_wq, (q_ - S_WQ) * (H * DH * 2), 0); \
    else if constexpr (q_ < S_V) ring[q_ % RING] = __builtin_amdgcn_raw_buffer_load_b128(r_k, o_kv, (q_ - S_K) * 1024, KV_AUX); \
    else if constexpr (q_ < S_WO) ring[q_ % RING] = __builtin_amdgcn_raw_buffer_load_b128(r_v, o_kv, (q_ - S_V) * 1024, KV_AUX); \
    else if constexpr (q_ < S_W2) ring[q_ % RING] = __builtin_amdgcn_raw_buffer_load_b128(r_wo, o_wo, (q_ - S_WO) * D * 2, 0); \
    else ring[q_ % RING] = __builtin_amdgcn_raw_buffer_load_b128(r_w2, o_w2, (q_ - S_W2) * D * 2, 0); } } while (0)
  auto issue = [&]<int I>() { ISSUE(I); };
  [&]<int... I>(std::integer_sequence<int, I...>) { (issue.template operator()<I>(), ...); }(std::make_integer_sequence<int, RING>{});
  __builtin_amdgcn_sched_barrier(0);

  {
    const float xf = bflo(xl);
    float hv[16];
#pragma unroll
    for (int k = 0; k < 4; ++k) { hv[2 * k] = xf * bflo(w1a[k]); hv[2 * k + 1] = xf * bfhi(w1a[k]); hv[8 + 2 * k] = xf * bflo(w1b[k]); hv[8 + 2 * k + 1] = xf * bfhi(w1b[k]); }
#pragma unroll
    for (int k = 0; k < 8; ++k) hv[k] = swap32_add(hv[k], hv[k + 8]);
#pragma unroll
    for (int k = 0; k < 4; ++k) hv[k] = swap16_add(hv[k], hv[k + 4]);
#pragma unroll
    for (int k = 0; k < 2; ++k) { const float t1 = hv[k] + dpp<XOR8>(hv[k]), t2 = hv[k + 2] + dpp<XOR8>(hv[k + 2]); hv[k] = (lane & 8) ? t2 : t1; }
    { const float t1 = hv[0] + dpp<XOR7>(hv[0]), t2 = hv[1] + dpp<XOR7>(hv[1]); hv[0] = (lane & 4) ? t2 : t1; }
    hv[0] += dpp<XOR2>(hv[0]); hv[0] += dpp<XOR1>(hv[0]);
    if ((lane & 3) == 0) L_hp[w * 16 + (lane >> 2)] = hv[0];
  }
  __builtin_amdgcn_sched_barrier(0);
  {
    float qa[8];
#pragma unroll
    for (int k = 0; k < 8; ++k) qa[k] = 0.f;
    [&]<int... I>(std::integer_sequence<int, I...>) {
      ([&] {
        const v4u r0 = ring[(2 * I) % RING], r1 = ring[(2 * I + 1) % RING];
#pragma unroll
        for (int k = 0; k < 4; ++k) { qa[2 * k] = dot2(perm_lo(r0[k], r1[k]), xp[I], qa[2 * k]); qa[2 * k + 1] = dot2(perm_hi(r0[k], r1[k]), xp[I], qa[2 * k + 1]); }
        __builtin_amdgcn_sched_barrier(0);
        issue.template operator()<2 * I + RING>(); issue.template operator()<2 * I + 1 + RING>();
        __builtin_amdgcn_sched_barrier(0);
      }(), ...);
    }(std::make_integer_sequence<int, 14>{});
    float* dst = L_part + (w * 2 + hh) * 256 + 8 * c;
    *reinterpret_cast<f32x4*>(dst) = f32x4{qa[0], qa[1], qa[2], qa[3]};
    *reinterpret_cast<f32x4*>(dst + 4) = f32x4{qa[4], qa[5], qa[6], qa[7]};
  }
  __syncthreads();
  if (tid < 256) {
    float t = 0.f;
#pragma unroll
    for (int r = 0; r < 16; ++r) t += L_part[r * 256 + tid];
    const float o = dpp<XOR1>(t);
    if ((tid & 1) == 0) L_q[tid >> 1] = cvtpk(t, o);
  } else if (tid < 272) {
    float t = bflo(b1v);
#pragma unroll
    for (int r = 0; r < 8; ++r) t += L_hp[r * 16 + (tid - 256)];
    t = fmaxf(t, 0.f);
    L_h[tid - 256] = t * t;
  }
  __syncthreads();
  const v4u qv = *reinterpret_cast<const v4u*>(L_q + 4 * c);
  float st[6];
  [&]<int... I>(std::integer_sequence<int, I...>) {
    ([&] {
      const v4u r = ring[(S_K + I) % RING];
      float d = dot2(r[0], qv[0], 0.f); d = dot2(r[1], qv[1], d); d = dot2(r[2], qv[2], d); d = dot2(r[3], qv[3], d);
      __builtin_amdgcn_sched_barrier(0);
      issue.template operator()<S_K + I + RING>();
      __builtin_amdgcn_sched_barrier(0);
      if constexpr ((I & 1) == 0) { st[0] = d; } else {
        d = swap16_add(st[0], d);
        if constexpr ((I & 2) == 0) { st[1] = d; } else {
          { const float t1 = st[1] + dpp<XOR8>(st[1]), t2 = d + dpp<XOR8>(d); d = (c & 8) ? t2 : t1; }
          if constexpr ((I & 4) == 0) { st[2] = d; } else {
            { const float t1 = st[2] + dpp<XOR7>(st[2]), t2 = d + dpp<XOR7>(d); d = (c & 4) ? t2 : t1; }
            if constexpr ((I & 8) == 0) { st[3] = d; } else {
              { const float t1 = st[3] + dpp<XOR2>(st[3]), t2 = d + dpp<XOR2>(d); d = (c & 2) ? t2 : t1; }
              if constexpr ((I & 16) == 0) { st[4] = d; } else {
                { const float t1 = st[4] + dpp<XOR1>(st[4]), t2 = d + dpp<XOR1>(d); st[5] = (c & 1) ? t2 : t1; }
              } } } } }
    }(), ...);
  }(std::make_integer_sequence<int, 32>{});
  constexpr float C2 = 0.0625f * 1.4426950408889634f;
  const float s2 = st[5] * C2;
  const float mw = wave_max(s2);
  const float p = __builtin_amdgcn_exp2f(s2 - mw);
  const float lw = wave_sum(p);
  L_p[w * 64 + hh * 32 + (__builtin_bitreverse32((unsigned)c) >> 27)] = p;
  if (lane == 0) { L_ml[w] = mw; L_ml[8 + w] = lw; }
  __builtin_amdgcn_fence(__ATOMIC_RELEASE, "wavefront");
  __builtin_amdgcn_wave_barrier();
  __builtin_amdgcn_fence(__ATOMIC_ACQUIRE, "wavefront");
  float acc[8];
#pragma unroll
  for (int k = 0; k < 8; ++k) acc[k] = 0.f;
  f32x4 pq;
  [&]<int... I>(std::integer_sequence<int, I...>) {
    ([&] {
      if constexpr ((I & 3) == 0) pq = *reinterpret_cast<const f32x4*>(L_p + w * 64 + hh * 32 + I);
      const v4u r = ring[(S_V + I) % RING];
      const float pp = pq[I & 3];
#pragma unroll
      for (int k = 0; k < 4; ++k) { acc[2 * k] = fmaf(pp, bflo(r[k]), acc[2 * k]); acc[2 * k + 1] = fmaf(pp, bfhi(r[k]), acc[2 * k + 1]); }
      asm volatile("" : "+v"(acc[0]), "+v"(acc[1]), "+v"(acc[2]), "+v"(acc[3]), "+v"(acc[4]), "+v"(acc[5]), "+v"(acc[6]), "+v"(acc[7]));
      __builtin_amdgcn_sched_barrier(0);
      issue.template operator()<S_V + I + RING>();
      __builtin_amdgcn_sched_barrier(0);
    }(), ...);
  }(std::make_integer_sequence<int, 32>{});
  {
    float* dst = L_part + (w * 2 + hh) * 256 + 8 * c;
    *reinterpret_cast<f32x4*>(dst) = f32x4{acc[0], acc[1], acc[2], acc[3]};
    *reinterpret_cast<f32x4*>(dst + 4) = f32x4{acc[4], acc[5], acc[6], acc[7]};
  }
  [&]<int... I>(std::integer_sequence<int, I...>) {
    ([&] {
      const v4u r0 = ring[(S_WO + 2 * I) % RING], r1 = ring[(S_WO + 2 * I + 1) % RING];
      v4u n0, n1;
      n0[0] = perm_lo(r0[0], r1[0]); n0[1] = perm_hi(r0[0], r1[0]); n0[2] = perm_lo(r0[1], r1[1]); n0[3] = perm_hi(r0[1], r1[1]);
      n1[0] = perm_lo(r0[2], r1[2]); n1[1] = perm_hi(r0[2], r1[2]); n1[2] = perm_lo(r0[3], r1[3]); n1[3] = perm_hi(r0[3], r1[3]);
      ring[(S_WO + 2 * I) % RING] = n0; ring[(S_WO + 2 * I + 1) % RING] = n1;
    }(), ...);
  }(std::make_integer_sequence<int, 17>{});
  __syncthreads();
  float M, Lsum;
  unsigned opk;
  {
    const f32x4 m0 = *reinterpret_cast<const f32x4*>(L_ml), m1 = *reinterpret_cast<const f32x4*>(L_ml + 4);
    const f32x4 l0 = *reinterpret_cast<const f32x4*>(L_ml + 8), l1 = *reinterpret_cast<const f32x4*>(L_ml + 12);
    M = fmaxf(fmaxf(fmaxf(m0[0], m0[1]), fmaxf(m0[2], m0[3])), fmaxf(fmaxf(m1[0], m1[1]), fmaxf(m1[2], m1[3])));
    const f32x4 mm = hh ? m1 : m0, ll = hh ? l1 : l0;
    float o = 0.f, ls = 0.f;
#pragma unroll
    for (int k = 0; k < 4; ++k) {
      const float f = __builtin_amdgcn_exp2f(mm[k] - M);
      const float* src = L_part + ((4 * hh + k) * 2) * 256 + 32 * w + c;
      o = fmaf(f, src[0] + src[256], o); ls = fmaf(f, ll[k], ls);
    }
    o = swap32_add(o, o); Lsum = swap32_add(ls, ls);
    opk = cvtpk(o, dpp<XOR1>(o));
  }
  float ya[8], yf[8];
#pragma unroll
  for (int k = 0; k < 8; ++k) ya[k] = 0.f;
  [&]<int... I>(std::integer_sequence<int, I...>) {
    ([&] {
      const unsigned op = __builtin_amdgcn_readlane(opk, 2 * I);
      const v4u r0 = ring[(S_WO + 2 * I) % RING], r1 = ring[(S_WO + 2 * I + 1) % RING];
#pragma unroll
      for (int k = 0; k < 4; ++k) { ya[k] = dot2(r0[k], op, ya[k]); ya[4 + k] = dot2(r1[k], op, ya[4 + k]); }
    }(), ...);
  }(std::make_integer_sequence<int, 16>{});
  {
    const unsigned hp = cvtpk(L_h[2 * w], L_h[2 * w + 1]);
    const v4u r0 = ring[S_W2 % RING], r1 = ring[(S_W2 + 1) % RING];
#pragma unroll
    for (int k = 0; k < 4; ++k) { yf[k] = dot2(r0[k], hp, 0.f); yf[4 + k] = dot2(r1[k], hp, 0.f); }
  }
  if (lane < 56) {
    float* dst = L_y + w * D + 8 * lane;
    *reinterpret_cast<f32x4*>(dst) = f32x4{ya[0], ya[1], ya[2], ya[3]};
    *reinterpret_cast<f32x4*>(dst + 4) = f32x4{ya[4], ya[5], ya[6], ya[7]};
    dst += 8 * D;
    *reinterpret_cast<f32x4*>(dst) = f32x4{yf[0], yf[1], yf[2], yf[3]};
    *reinterpret_cast<f32x4*>(dst + 4) = f32x4{yf[4], yf[5], yf[6], yf[7]};
  }
  __syncthreads();
  const int pj = b * 128 + j;
  if (tid < 224) {
    const int which = tid >= 112, col4 = (tid - which * 112) * 4;
    f32x4 t = {0.f, 0.f, 0.f, 0.f};
#pragma unroll
    for (int r = 0; r < 8; ++r) t += *reinterpret_cast<const f32x4*>(L_y + (which * 8 + r) * D + col4);
    *reinterpret_cast<f32x4*>(a.ws + (which ? WS_YF : WS_YA) + pj * D + col4) = t;
  } else if (tid == 256) {
    a.ws[WS_ML + 2 * pj] = M; a.ws[WS_ML + 2 * pj + 1] = Lsum;
  }
}

__global__ void __launch_bounds__(256) kB(Args a) {
  __shared__ __attribute__((aligned(16))) float red[2][4][16];
  __shared__ float gred[4];
  const int b = blockIdx.x / 28, ch = blockIdx.x % 28, tid = threadIdx.x, lane = tid & 63, w = tid >> 6;
  const int pr = tid >> 2, c4 = tid & 3, col = ch * 16 + c4 * 4;
  f32x4 va[2], vf[2], ml[2][4];
#pragma unroll
  for (int ps = 0; ps < 2; ++ps) {
    const int j = pr + 64 * ps, pj = b * 128 + j;
    va[ps] = *reinterpret_cast<const f32x4*>(a.ws + WS_YA + pj * D + col);
    vf[ps] = *reinterpret_cast<const f32x4*>(a.ws + WS_YF + pj * D + col);
#pragma unroll
    for (int k = 0; k < 4; ++k) ml[ps][k] = *reinterpret_cast<const f32x4*>(a.ws + WS_ML + 2 * (pj & ~7) + 4 * k);
  }
  float gp = 0.f;
  if (tid < 224) { const unsigned xv = reinterpret_cast<const unsigned*>(a.x + b * D)[tid], gv = reinterpret_cast<const unsigned*>(a.gw)[tid]; gp = dot2(xv, gv, 0.f); }
  f32x4 sa = {0.f, 0.f, 0.f, 0.f}, sf = vf[0] + vf[1];
#pragma unroll
  for (int ps = 0; ps < 2; ++ps) {
    float M = -INFINITY;
#pragma unroll
    for (int k = 0; k < 4; ++k) M = fmaxf(M, fmaxf(ml[ps][k][0], ml[ps][k][2]));
    float L = 0.f, mine = 0.f;
#pragma unroll
    for (int k = 0; k < 4; ++k) {
      const float e0 = __builtin_amdgcn_exp2f(ml[ps][k][0] - M), e1 = __builtin_amdgcn_exp2f(ml[ps][k][2] - M);
      L = fmaf(e0, ml[ps][k][1], L); L = fmaf(e1, ml[ps][k][3], L);
      if ((pr & 7) == 2 * k) mine = e0; if ((pr & 7) == 2 * k + 1) mine = e1;
    }
    sa += va[ps] * (mine / L);
  }
#pragma unroll
  for (int k = 0; k < 4; ++k) {
#pragma unroll
    for (int m = 4; m < 64; m <<= 1) { sa[k] += __shfl_xor(sa[k], m); sf[k] += __shfl_xor(sf[k], m); }
  }
  gp = wave_sum(gp);
  if (lane < 4) { *reinterpret_cast<f32x4*>(&red[0][w][lane * 4]) = sa; *reinterpret_cast<f32x4*>(&red[1][w][lane * 4]) = sf; }
  if (lane == 0) gred[w] = gp;
  __syncthreads();
  if (tid < 16) {
    const float A = red[0][0][tid] + red[0][1][tid] + red[0][2][tid] + red[0][3][tid];
    const float Bv = red[1][0][tid] + red[1][1][tid] + red[1][2][tid] + red[1][3][tid] + bflo(a.b2[ch * 16 + tid]);
    const float z = gred[0] + gred[1] + gred[2] + gred[3];
    const float g = 1.f / (1.f + __expf(-z));
    const float o = g * A + (1.f - g) * Bv;
    a.out[b * D + ch * 16 + tid] = (unsigned short)(cvtpk(o, o) & 0xffffu);
  }
}

extern "C" void kernel_launch(void* const* d_in, const int* in_sizes, int n_in, void* d_out, int out_size, void* d_ws, size_t ws_size, hipStream_t stream) {
  Args a;
  a.x = (const unsigned short*)d_in[0]; a.kc = (const unsigned short*)d_in[1]; a.vc = (const unsigned short*)d_in[2];
  a.wq = (const unsigned short*)d_in[3]; a.wo = (const unsigned short*)d_in[4];
  a.w1 = (const unsigned short*)d_in[6]; a.b1 = (const unsigned short*)d_in[7]; a.w2 = (const unsigned short*)d_in[8]; a.b2 = (const unsigned short*)d_in[9];
  a.gw = (const unsigned short*)d_in[10];
  a.ws = (float*)d_ws; a.out = (unsigned short*)d_out;
  hipLaunchKernelGGL(kA, dim3(256), dim3(512), 0, stream, a);
  hipLaunchKernelGGL(kB, dim3(56), dim3(256), 0, stream, a);
}
```

```cpp
#include <hip/hip_runtime.h>
#include <stdint.h>
#include <utility>

typedef unsigned v4u __attribute__((ext_vector_type(4)));
typedef unsigned v3u __attribute__((ext_vector_type(3)));
typedef unsigned v2u __attribute__((ext_vector_type(2)));
typedef float f32x4 __attribute__((ext_vector_type(4)));
typedef __bf16 bf16x2 __attribute__((ext_vector_type(2)));
typedef __amdgpu_buffer_rsrc_t rsrc_t;
#define LAS __attribute__((address_space(3)))

constexpr int D = 448, H = 16, SK = 4096, DH = 256, F = 1792, KEYS = 512;
constexpr int PACE = 438, POFF = 112;
constexpr int WIN = 4;
constexpr int START = 20, CR = 4;
constexpr int W2DT = 12;
constexpr int SWIN = 6;
constexpr int QSLEEP = 40, OSLEEP = 32, YDELAY = 14, YSLEEP = 5;
constexpr int wo_step(int n) { return 8 + n * 48 / 15; }
constexpr int PR[3] = {12, 13, 15}, PW[3] = {9, 16, 17}, PT[5] = {11, 10, 14, 8, 7};
constexpr unsigned PX[8] = {0x4000, 0, 0, 0, 0x100, 0x880, 0x80, 0x80};
static_assert(((PX[0] | PX[1] | PX[2]) & 0x180) == 0, "quarter order is per (batch, head)");
__host__ __device__ constexpr unsigned spread(const int* bits, int n, unsigned v) { unsigned o = 0; for (int i = 0; i < n; ++i) o |= ((v >> i) & 1u) << bits[i]; return o; }
constexpr int slab_of(int J) { return (spread(PT, 5, J * 8) >> 7) & 3; }
constexpr int consumed_before(int i) { const int a = i <= START ? 0 : CR * (i - START), b = i - WIN + 1; return b < 0 ? 0 : a < b ? a : b; }
constexpr int wo_between(int lo, int hi) { int n = 0; for (int k = 0; k < 16; ++k) n += (wo_step(k) >= lo && wo_step(k) < hi); return n; }

constexpr int SC1 = 16, NT = 2;
constexpr unsigned OOB = 0x40000000u;
constexpr unsigned SPIN_CAP = 1u << 20;
constexpr unsigned OREC = 768;
constexpr unsigned WS_EPOCH = 0, WS_Q = 4096, WS_H = WS_Q + 16 * 2 * 4 * 256 * 4, WS_O = WS_H + 28 * 2 * 8 * 64 * 4, WS_Y = WS_O + 256 * OREC, WS_END = WS_Y + 256 * D * 4;
constexpr int L_Q = 0, L_ML = 128, L_O = L_ML + 16, L_Y = L_O + 4 * 8 * 72, L_MISC = L_Y + 9 * D, L_RED = L_MISC + 16, L_CNT = L_RED + 8 * 32, L_END = L_CNT + 8;
constexpr int C_Q = 0, C_O1 = 1, C_O2 = 2, C_S = 3;
constexpr int NCONS = 28;

__device__ __forceinline__ float dot2(unsigned a, unsigned b, float acc) {
  return __builtin_amdgcn_fdot2_f32_bf16(__builtin_bit_cast(bf16x2, a), __builtin_bit_cast(bf16x2, b), acc, false);
}
__device__ __forceinline__ float dot8(const v4u& a, const v4u& b, float acc) { return dot2(a[3], b[3], dot2(a[2], b[2], dot2(a[1], b[1], dot2(a[0], b[0], acc)))); }
__device__ __forceinline__ unsigned cvtpk(float lo, float hi) { return __builtin_bit_cast(unsigned, bf16x2{(__bf16)lo, (__bf16)hi}); }
template <int CTRL> __device__ __forceinline__ float dpp(float x) {
  return __builtin_bit_cast(float, __builtin_amdgcn_mov_dpp(__builtin_bit_cast(int, x), CTRL, 0xf, 0xf, true));
}
constexpr int XOR1 = 0xB1, XOR2 = 0x4E, XOR7 = 0x141, XOR8 = 0x128;
__device__ __forceinline__ float swap16_add(float a, float b) {
  auto s = __builtin_amdgcn_permlane16_swap(__float_as_uint(a), __float_as_uint(b), false, false);
  return __uint_as_float(s[0]) + __uint_as_float(s[1]);
}
__device__ __forceinline__ float swap32_add(float a, float b) {
  auto s = __builtin_amdgcn_permlane32_swap(__float_as_uint(a), __float_as_uint(b), false, false);
  return __uint_as_float(s[0]) + __uint_as_float(s[1]);
}
__device__ __forceinline__ float swap8_add(float a, float b, bool hi) {
  const float t1 = a + dpp<XOR8>(a), t2 = b + dpp<XOR8>(b); return hi ? t2 : t1;
}
__device__ __forceinline__ float sum_lo3(float x) { x += dpp<XOR1>(x); x += dpp<XOR2>(x); return x + dpp<XOR7>(x); }
__device__ __forceinline__ float max_lo3(float x) { x = fmaxf(x, dpp<XOR1>(x)); x = fmaxf(x, dpp<XOR2>(x)); return fmaxf(x, dpp<XOR7>(x)); }
__device__ __forceinline__ float sum_hi3(float x) { x += dpp<XOR8>(x); x = swap16_add(x, x); return swap32_add(x, x); }
__device__ __forceinline__ float max_hi3(float x) {
  x = fmaxf(x, dpp<XOR8>(x));
  auto s = __builtin_amdgcn_permlane16_swap(__float_as_uint(x), __float_as_uint(x), false, false);
  x = fmaxf(__uint_as_float(s[0]), __uint_as_float(s[1]));
  auto t = __builtin_amdgcn_permlane32_swap(__float_as_uint(x), __float_as_uint(x), false, false);
  return fmaxf(__uint_as_float(t[0]), __uint_as_float(t[1]));
}
__device__ __forceinline__ rsrc_t mk(const void* p, unsigned bytes) {
  return __builtin_amdgcn_make_buffer_rsrc(const_cast<void*>(p), 0, (int)bytes, 0x00020000);
}
template <int AUX> __device__ __forceinline__ v4u ld16(rsrc_t r, unsigned voff, unsigned soff) { return __builtin_amdgcn_raw_buffer_load_b128(r, voff, soff, AUX); }
__device__ __forceinline__ float bflo(unsigned u) { return __uint_as_float(u << 16); }
__device__ __forceinline__ float bfhi(unsigned u) { return __uint_as_float(u & 0xffff0000u); }
__device__ __forceinline__ unsigned perm_lo(unsigned a, unsigned b) { return __builtin_amdgcn_perm(b, a, 0x05040100u); }
__device__ __forceinline__ unsigned perm_hi(unsigned a, unsigned b) { return __builtin_amdgcn_perm(b, a, 0x07060302u); }
__device__ __forceinline__ void pair_rows(v4u& r0, v4u& r1) {
  v4u n0, n1;
  n0[0] = perm_lo(r0[0], r1[0]); n0[1] = perm_hi(r0[0], r1[0]); n0[2] = perm_lo(r0[1], r1[1]); n0[3] = perm_hi(r0[1], r1[1]);
  n1[0] = perm_lo(r0[2], r1[2]); n1[1] = perm_hi(r0[2], r1[2]); n1[2] = perm_lo(r0[3], r1[3]); n1[3] = perm_hi(r0[3], r1[3]);
  asm volatile("" : "+v"(n0), "+v"(n1)); r0 = n0; r1 = n1;
}
__device__ __forceinline__ void mac_pair(float (&y)[8], const v4u& n0, const v4u& n1, unsigned c) {
#pragma unroll
  for (int k = 0; k < 4; ++k) { y[k] = dot2(n0[k], c, y[k]); y[4 + k] = dot2(n1[k], c, y[4 + k]); }
}
__device__ __forceinline__ void st8(float* dst, const float (&y)[8]) {
  *reinterpret_cast<f32x4*>(dst) = f32x4{y[0], y[1], y[2], y[3]}; *reinterpret_cast<f32x4*>(dst + 4) = f32x4{y[4], y[5], y[6], y[7]};
}
template <int K> __device__ __forceinline__ unsigned tagw(float v, unsigned T) { return __builtin_amdgcn_perm(__float_as_uint(v), T, 0x07060500u | K); }
__device__ __forceinline__ v4u tag4(float a, float b, float c, float d, unsigned T) { return v4u{tagw<0>(a, T), tagw<1>(b, T), tagw<2>(c, T), tagw<3>(d, T)}; }
__device__ __forceinline__ float untag(unsigned u) { return __uint_as_float(u & 0xffffff00u); }
__device__ __forceinline__ unsigned tags4(v4u g) { return __builtin_amdgcn_perm(__builtin_amdgcn_perm(g[3], g[2], 0x04000400u), __builtin_amdgcn_perm(g[1], g[0], 0x04000400u), 0x05040100u); }
#define SB() __builtin_amdgcn_sched_barrier(0)
#define FENCE() asm volatile("" ::: "memory")
#define VMCNT(n) asm volatile("s_waitcnt vmcnt(%0)" :: "i"(n) : "memory")
#define PIN8(a) asm volatile("" : "+v"(a[0]), "+v"(a[1]), "+v"(a[2]), "+v"(a[3]), "+v"(a[4]), "+v"(a[5]), "+v"(a[6]), "+v"(a[7]))
__device__ __forceinline__ void bar() { SB(); __syncthreads(); SB(); }
template <int TICKS> __device__ __forceinline__ void wait_until(unsigned t0) {
  asm volatile("s_branch 2f\n1:\n\ts_sleep 1\n2:\n\ts_memrealtime vcc\n\ts_waitcnt lgkmcnt(0)\n\ts_sub_u32 vcc_lo, vcc_lo, %0\n\ts_cmpk_lt_u32 vcc_lo, %1\n\ts_cbranch_scc1 1b"
               :: "s"(t0), "i"(TICKS) : "vcc", "scc", "memory");
}
template <int N, class Fn, int... I> __device__ __forceinline__ void unroll_(Fn&& f, std::integer_sequence<int, I...>) { (f(std::integral_constant<int, I>{}), ...); }
template <int N, class Fn> __device__ __forceinline__ void unroll(Fn&& f) { unroll_<N>(f, std::make_integer_sequence<int, N>{}); }
__device__ __forceinline__ void lds_signal(float* lds, int k, int lane) {
  FENCE();
  if (lane == 0) __hip_atomic_fetch_add((LAS unsigned*)(lds + L_CNT + k), 1u, __ATOMIC_RELAXED, __HIP_MEMORY_SCOPE_WORKGROUP);
  FENCE();
}
template <int SLEEP> __device__ __forceinline__ void lds_wait(float* lds, int k, unsigned n) {
  while (__builtin_amdgcn_readfirstlane(__hip_atomic_load((LAS unsigned*)(lds + L_CNT + k), __ATOMIC_RELAXED, __HIP_MEMORY_SCOPE_WORKGROUP)) < n) { if (SLEEP) __builtin_amdgcn_s_sleep(SLEEP); }
  FENCE();
}

struct Args {
  const unsigned short *x, *kc, *vc, *wq, *wo, *w1, *b1, *w2, *b2, *gw;
  unsigned char* ws; unsigned short* out;
};

__global__ void __launch_bounds__(576, 3) fused(Args a) {
  __shared__ __attribute__((aligned(16))) float lds[L_END];
  const int bid = blockIdx.x, xcd = bid & 7, idx = bid >> 3;
  const int h = xcd * 2 + (idx >> 4), b = (idx >> 3) & 1, s = idx & 7;
  const int j = h * 8 + s;
  const int tid = threadIdx.x, lane = tid & 63, wv = __builtin_amdgcn_readfirstlane(tid >> 6), w = wv - 1;
  const int r8 = lane >> 3, c8 = lane & 7;
  unsigned cpf = 0;
  if (wv == 0) {
    unsigned long long pc; unsigned len;
    asm volatile("s_getpc_b64 %0\n.Lcode_a:\n\ts_mov_b32 %1, .Lcode_z-.Lcode_a" : "=s"(pc), "=s"(len));
    cpf = __builtin_amdgcn_raw_buffer_load_b32(mk(reinterpret_cast<const void*>(pc), len), lane < 8 ? (lane * 32 + idx) * 128 : OOB, 0, 0); SB(); }
  const unsigned lane56 = lane < 56 ? 0u : OOB;
  const unsigned o_row = lane * 16 + lane56;

  auto tile_xor = [&]() { unsigned id = s | b << 3 | h << 4, x = 0; asm volatile("" : "+s"(id));
#pragma unroll
    for (int k = 0; k < 8; ++k) x ^= ((id >> k) & 1) ? PX[k] : 0u;
    return x; };

  float Tv = 0.f, base = 0.f, bias = 0.f; unsigned tsel = 0, o_y = 0, o_p = 0, o_r = 0, o_out = 0, l_b = 0, l_y = 0;
  const int cb = bid >= 14, ch = bid - 14 * cb;
  auto pre_addr = [&]() {
    tsel = 0x07060500u | (lane & 3); o_y = WS_Y + ((b * 128 + j) * D + tid) * 4;
    o_p = WS_Y + ((cb * 128 + w * 16 + r8) * D + ch * 32) * 4 + c8 * 16;
    o_r = L_RED + w * 32 + 4 * c8 + (lane >> 4); l_b = L_Y + (tid < D ? tid : 0); l_y = L_Y + w * D + 8 * lane;
    o_out = (lane & 33) ? OOB : (bid * 32 + lane) * 2;
    asm volatile("" : "+v"(tsel), "+v"(o_y), "+v"(o_p), "+v"(o_r), "+v"(o_out), "+v"(l_b), "+v"(l_y)); };
  auto pre_tail = [&]() { SB(); Tv = lds[L_MISC + 1]; base = lds[l_b + 8 * D]; SB(); };

  if (wv > 0) {
    v4u wrm[3];
    unsigned o_wrm = o_row + (h * DH + (idx & 15) * 16 + w * 2) * D * 2;
    asm volatile("" : "+v"(o_wrm)); SB();
    { const rsrc_t r_all = mk(a.wo, H * DH * D * 2); wrm[0] = ld16<0>(r_all, o_wrm, 0); wrm[1] = ld16<0>(r_all, o_wrm + D * 2, 0); }
    const unsigned t0 = (unsigned)__builtin_amdgcn_s_memrealtime();
    FENCE(); SB();
    const size_t kvoff = ((size_t)(b * H + h) * SK + (size_t)s * KEYS) * DH;
    const rsrc_t r_k = mk(a.kc + kvoff, KEYS * DH * 2), r_v = mk(a.vc + kvoff, KEYS * DH * 2), r_w2 = mk(a.w2, F * D * 2), r_wo = mk(a.wo + (size_t)h * DH * D, DH * D * 2);
    const unsigned o_kv = spread(PR, 3, r8) | spread(PW, 3, w) | c8 * 16, xo = tile_xor(), sx = (xo >> 7) & 3;
    v4u kv[64], wo[2][8], qv[4], w2r[2];
    float p[8], y[8], acc[8], fw = 0.f, mw = -3.0e38f; unsigned hp = 0;
#pragma unroll
    for (int k = 0; k < 8; ++k) { y[k] = 0.f; p[k] = 0.f; }
    auto project = [&](auto J_) { constexpr int J = J_;
      if constexpr (J == 2) {
        lds_wait<0>(lds, C_O2, 8); lds_wait<0>(lds, C_S, 1);
        fw = lds[L_MISC] * __builtin_amdgcn_exp2f(lds[L_ML + r8] - lds[L_MISC + 2]);
        hp = __builtin_amdgcn_readfirstlane(__float_as_uint(lds[L_MISC + 8 + w]));
      } else bar();
      const float oin = lds[L_O + (J * 8 + r8) * 72 + 8 * w + c8];
      if constexpr (J == 3) pre_tail();
      const float o = sum_hi3(fw * oin);
      const unsigned opk = cvtpk(o, dpp<XOR1>(o));
#pragma unroll
      for (int u = 0; u < 4; ++u) mac_pair(y, wo[J - 2][2 * u], wo[J - 2][2 * u + 1], __builtin_amdgcn_readlane(opk, 2 * u));
      PIN8(y); SB();
      if constexpr (J == 3) { pair_rows(w2r[0], w2r[1]); mac_pair(y, w2r[0], w2r[1], hp); PIN8(y); SB(); }
    };
    auto consume = [&](auto I_) { constexpr int I = I_, J = (I >> 3) & 3, Tt = I & 7;
      if constexpr (I == 0) { lds_wait<1>(lds, C_Q, 1);
#pragma unroll
        for (int k = 0; k < 4; ++k) qv[k] = *reinterpret_cast<const v4u*>(lds + L_Q + (slab_of(k) ^ sx) * 32 + 4 * c8); }
      const v4u r = kv[I];
      if constexpr (I < 32) {
        p[Tt] = dot8(r, qv[J], p[Tt]);
        if constexpr (J == 3) {
          const float d = sum_lo3(p[Tt]);
          p[Tt] = bflo(cvtpk(d, d)) * (0.0625f * 1.4426950408889634f); mw = fmaxf(mw, p[Tt]); asm volatile("" : "+v"(mw)); }
        asm volatile("" : "+v"(p[Tt]));
      } else {
        if constexpr (I == 32) {
          mw = max_hi3(mw);
#pragma unroll
          for (int k = 0; k < 8; ++k) p[k] = __builtin_amdgcn_exp2f(p[k] - mw);
        }
        if constexpr (Tt == 0) {
#pragma unroll
          for (int k = 0; k < 8; ++k) acc[k] = 0.f;
        }
#pragma unroll
        for (int k = 0; k < 4; ++k) { acc[2 * k] = fmaf(p[Tt], bflo(r[k]), acc[2 * k]); acc[2 * k + 1] = fmaf(p[Tt], bfhi(r[k]), acc[2 * k + 1]); }
        PIN8(acc);
        if constexpr (Tt == 7) {
#pragma unroll
          for (int k = 0; k < 4; ++k) acc[k] = swap32_add(acc[k], acc[k + 4]);
#pragma unroll
          for (int k = 0; k < 2; ++k) acc[k] = swap16_add(acc[k], acc[k + 2]);
          lds[L_O + (J * 8 + w) * 72 + 8 * c8 + r8] = swap8_add(acc[0], acc[1], lane & 8);
          if constexpr (J == 1) lds_signal(lds, C_O1, lane);
          if constexpr (J == 2) lds_signal(lds, C_O2, lane);
        }
      }
      SB(); };
    auto after = [&](auto I_) { constexpr int I = I_;
      if constexpr (I == 33) { const float lw = sum_hi3(((p[0] + p[1]) + (p[2] + p[3])) + ((p[4] + p[5]) + (p[6] + p[7])));
        if (lane == 0) { lds[L_ML + w] = mw; lds[L_ML + 8 + w] = lw; } SB(); }
      if constexpr (I >= 48 && I < 52) { pair_rows(wo[0][2 * (I - 48)], wo[0][2 * (I - 48) + 1]); SB(); }
      if constexpr (I >= 54 && I < 58) { pair_rows(wo[1][2 * (I - 54)], wo[1][2 * (I - 54) + 1]); SB(); }
      if constexpr (I == 52) { pre_addr(); SB(); }
      if constexpr (I == 59) project(std::integral_constant<int, 2>{});
      if constexpr (I == 63) project(std::integral_constant<int, 3>{}); };
    bar();
    wrm[2] = ld16<NT>(w == 0 ? r_k : w == 1 ? r_v : r_w2, lane == 0 && w < 3 ? (w == 2 ? 16 * j * D * 2 : 0) : OOB, 0);
    SB();
    unroll<64 + WIN>([&](auto I_) { constexpr int I = I_;
      constexpr int c0 = I == 0 ? 0 : consumed_before(I - 1), c1 = consumed_before(I) < 64 ? consumed_before(I) : 64;
      if constexpr (I == 16) asm volatile("" :: "v"(wrm[0]), "v"(wrm[1]), "v"(wrm[2]));
      unroll<c1 - c0>([&](auto U_) { consume(std::integral_constant<int, c0 + U_>{}); });
      if constexpr (I < 64) {
        if constexpr (c1 < I - WIN + 1) VMCNT(WIN - 1 + wo_between(I - WIN, I));
        wait_until<POFF + I * PACE / 16>(t0);
        kv[I] = ld16<NT>(I < 32 ? r_k : r_v, o_kv, spread(PT, 5, I & 31) ^ xo);
        if constexpr (wo_between(I, I + 1)) { constexpr int N = wo_between(0, I);
          wo[N >> 3][N & 7] = ld16<0>(r_wo, o_row + w * 8 * D * 2, ((slab_of(2 + (N >> 3)) ^ sx) * 64 + (N & 7)) * D * 2); }
        SB();
      }
      unroll<c1 - c0>([&](auto U_) { after(std::integral_constant<int, c0 + U_>{}); });
      if constexpr (I == 63) { wait_until<POFF + I * PACE / 16 + W2DT>(t0);
        w2r[0] = ld16<NT>(r_w2, o_row + (16 * j + 2 * w) * D * 2, 0); w2r[1] = ld16<NT>(r_w2, o_row + (16 * j + 2 * w + 1) * D * 2, 0); }
      SB();
    });
    if (lane < 56) st8(lds + l_y, y);
  } else {
    __builtin_amdgcn_s_setprio(3);
    const int cg = idx & 3, rq = (idx >> 2) & 3;
    const bool ffn = j < F / 16;
    const int rs = (j & 3) + 4 * b;
    unsigned o_wq = (rq * 112 + r8 * 14) * (H * DH * 2) + (h * DH + cg * 64 + c8 * 8) * 2, o_w1 = (rs * 56 + r8 * 8) * (F * 2) + ((j >> 2) * 64 + c8 * 8) * 2 + lane56;
    asm volatile("" : "+v"(o_wq), "+v"(o_w1)); SB();
    v4u xa[2], xf[2], t[14], t1[8]; v3u xb[2];
    const rsrc_t r_wq = mk(a.wq, D * H * DH * 2), r_w1 = mk(a.w1, ffn ? D * F * 2 : 0u);
#pragma unroll
    for (int k = 0; k < 14; ++k) t[k] = ld16<NT>(r_wq, o_wq, k * (H * DH * 2));
    FENCE(); SB();
    const rsrc_t r_ws = mk(a.ws, WS_END), r_x = mk(a.x, 2 * D * 2), r_wo = mk(a.wo + (size_t)h * DH * D, DH * D * 2);
    const unsigned E = __builtin_amdgcn_raw_buffer_load_b32(r_ws, WS_EPOCH, 0, 0);
#pragma unroll
    for (int bb = 0; bb < 2; ++bb) { const unsigned o = (bb * D + rq * 112 + r8 * 14) * 2;
      xa[bb] = ld16<0>(r_x, o, 0); xb[bb] = __builtin_amdgcn_raw_buffer_load_b96(r_x, o + 16, 0, 0); }
#pragma unroll
    for (int bb = 0; bb < 2; ++bb) xf[bb] = ld16<0>(r_x, (bb * D + rs * 56 + r8 * 8) * 2 + lane56, 0);
#pragma unroll
    for (int k = 0; k < 8; ++k) t1[k] = ld16<NT>(r_w1, o_w1, k * (F * 2));
    const v4u x0 = ld16<0>(r_x, o_row, 0), x1 = ld16<0>(r_x, o_row + D * 2, 0), gv = ld16<0>(mk(a.gw, D * 2), o_row, 0);
    const unsigned short b2v = __builtin_amdgcn_raw_buffer_load_b16(mk(a.b2, D * 2), (ch * 32 + (lane & 31)) * 2, 0, 0);
    const unsigned b1v = __builtin_amdgcn_raw_buffer_load_b32(mk(a.b1, F * 2), (16 * j + 2 * c8) * 2, 0, 0);
    if (lane < 8) lds[L_CNT + lane] = 0.f;
    bar();
    const unsigned T = __builtin_amdgcn_readfirstlane(E) + 0x01010101u;
    auto mac2 = [&](float (&v)[16], const v4u& r0, const v4u& r1, unsigned xp0, unsigned xp1) {
#pragma unroll
      for (int k = 0; k < 4; ++k) { const unsigned lo = perm_lo(r0[k], r1[k]), hi = perm_hi(r0[k], r1[k]);
        v[2 * k] = dot2(lo, xp0, v[2 * k]); v[2 * k + 1] = dot2(hi, xp0, v[2 * k + 1]); v[8 + 2 * k] = dot2(lo, xp1, v[8 + 2 * k]); v[9 + 2 * k] = dot2(hi, xp1, v[9 + 2 * k]); } };
    auto red16 = [&](float (&v)[16]) {
#pragma unroll
      for (int k = 0; k < 8; ++k) v[k] = swap32_add(v[k], v[k + 8]);
#pragma unroll
      for (int k = 0; k < 4; ++k) v[k] = swap16_add(v[k], v[k + 4]); };
    { float qa[16], ha[16];
#pragma unroll
      for (int k = 0; k < 16; ++k) { qa[k] = 0.f; ha[k] = 0.f; }
#pragma unroll
      for (int u = 0; u < 7; ++u) mac2(qa, t[2 * u], t[2 * u + 1], u < 4 ? xa[0][u & 3] : xb[0][u & 3], u < 4 ? xa[1][u & 3] : xb[1][u & 3]);
      red16(qa);
#pragma unroll
      for (int u = 0; u < 4; ++u) mac2(ha, t1[2 * u], t1[2 * u + 1], xf[0][u], xf[1][u]);
      red16(ha);
#pragma unroll
      for (int k = 0; k < 4; ++k) qa[k] = swap8_add(qa[k], ha[k], lane & 8);
      const unsigned oq = WS_Q + (((h * 2 + (lane >> 5)) * 4 + rq) * 256 + cg * 64) * 4, oh = ffn ? WS_H + ((((j >> 2) * 2 + (lane >> 5)) * 8 + rs) * 64) * 4 : OOB;
      __builtin_amdgcn_raw_buffer_store_b128(tag4(qa[0], qa[1], qa[2], qa[3], T), r_ws, ((lane & 8) ? oh : oq) + (c8 * 8 + ((lane >> 4) & 1) * 4) * 4, 0, SC1); }
    SB();
    const float g0 = 1.f / (1.f + __expf(-sum_hi3(sum_lo3(dot8(x0, gv, 0.f))))), g1 = 1.f / (1.f + __expf(-sum_hi3(sum_lo3(dot8(x1, gv, 0.f)))));
    const float g = b ? g1 : g0;
    bias = (1.f - (cb ? g1 : g0)) * bflo(b2v);
    asm volatile("" : "+v"(bias) : "v"(cpf));
    SB();
    __builtin_amdgcn_s_sleep(QSLEEP);
    { v4u gq[4]; v2u hg;
      const unsigned want = (c8 & 1) ? T >> 16 : T & 0xffffu;
      const unsigned o_h = ffn ? WS_H + ((((j >> 2) * 2 + b) * 8 + r8) * 64 + (j & 3) * 16 + 2 * c8) * 4 : OOB;
      for (unsigned spin = 0;; ++spin) { FENCE();
#pragma unroll
        for (int k = 0; k < 4; ++k) gq[k] = ld16<SC1>(r_ws, WS_Q + (h * 2 + b) * 4096 + lane * 16, k * 1024);
        hg = __builtin_amdgcn_raw_buffer_load_b64(r_ws, o_h, 0, SC1);
        bool ok = !ffn || __builtin_amdgcn_perm(hg[1], hg[0], 0x0c0c0400u) == want;
#pragma unroll
        for (int k = 0; k < 4; ++k) ok &= tags4(gq[k]) == T;
        if (__all(ok) || spin > SPIN_CAP) break;
        __builtin_amdgcn_s_sleep(4);
      }
      float q[4];
#pragma unroll
      for (int k = 0; k < 4; ++k) q[k] = (untag(gq[0][k]) + untag(gq[1][k])) + (untag(gq[2][k]) + untag(gq[3][k]));
      *reinterpret_cast<v2u*>(lds + L_Q + 2 * lane) = v2u{cvtpk(q[0], q[1]), cvtpk(q[2], q[3])};
      lds_signal(lds, C_Q, lane);
      __builtin_amdgcn_s_setprio(0);
      const float h0 = fmaxf(sum_hi3(untag(hg[0])) + bflo(b1v), 0.f), h1 = fmaxf(sum_hi3(untag(hg[1])) + bfhi(b1v), 0.f);
      if (lane < 8) lds[L_MISC + 8 + lane] = __uint_as_float(ffn ? cvtpk(h0 * h0 * (1.f - g), h1 * h1 * (1.f - g)) : 0u); }
    const unsigned sx = (tile_xor() >> 7) & 3;
    v4u wx[16];
    unroll<16 + SWIN>([&](auto K_) { constexpr int K = K_, C = K - SWIN;
      if constexpr (C >= 0 && (C & 1)) pair_rows(wx[C - 1], wx[C]);
      else if constexpr (C >= 0 && K < 16) VMCNT(SWIN - 1);
      SB();
      if constexpr (K < 16) wx[K] = ld16<0>(r_wo, o_row + s * 8 * D * 2, ((slab_of(K >> 3) ^ sx) * 64 + (K & 7)) * D * 2);
      SB(); });
    if (bid == 127) {
      for (unsigned spin = 0;; ++spin) { FENCE(); bool ok = true;
#pragma unroll
        for (int k = 0; k < 8; ++k) { const int wg = k * 32 + (lane >> 1);
          ok &= tags4(ld16<SC1>(r_ws, WS_Q + ((((wg >> 4) * 2) * 4 + ((wg >> 2) & 3)) * 256 + (wg & 3) * 64) * 4 + (lane & 1) * 16, 0)) == T; }
        if (__all(ok) || spin > SPIN_CAP) break;
        __builtin_amdgcn_s_sleep(8); }
      if (lane < 8) __builtin_amdgcn_raw_buffer_store_b128(v4u{T, T, T, T}, r_ws, WS_EPOCH + lane * 16, 0, SC1);
    }
    const unsigned o_grp = WS_O + (b * 16 + h) * 8 * OREC;
    lds_wait<4>(lds, C_O1, 8);
    __builtin_amdgcn_s_setprio(3);
    { const float mv = lds[L_ML + c8], lv = lds[L_ML + 8 + c8], M = max_lo3(mv);
      const float f = __builtin_amdgcn_exp2f(mv - M), L = sum_lo3(lv * f);
      f32x4 o = {0.f, 0.f, 0.f, 0.f};
#pragma unroll
      for (int k = 0; k < 8; ++k)
        o += *reinterpret_cast<const f32x4*>(lds + L_O + (((lane >> 4) & 1) * 8 + k) * 72 + 4 * (lane & 15)) * __builtin_bit_cast(float, __builtin_amdgcn_readlane(__builtin_bit_cast(int, f), k));
      const v4u rec = lane < 32 ? tag4(o[0], o[1], o[2], o[3], T) : v4u{__float_as_uint(M), T, __float_as_uint(L), T};
      if (lane < 40) __builtin_amdgcn_raw_buffer_store_b128(rec, r_ws, o_grp + s * OREC + lane * 16, 0, SC1); }
    SB();
    __builtin_amdgcn_s_sleep(OSLEEP);
    { v4u ml; unsigned o0, o1;
      const unsigned want = (T >> (8 * (c8 & 3))) & 0xffu;
      for (unsigned spin = 0;; ++spin) { FENCE();
        ml = ld16<SC1>(r_ws, o_grp + r8 * OREC + 512, 0);
        o0 = __builtin_amdgcn_raw_buffer_load_b32(r_ws, o_grp + r8 * OREC + (8 * s + c8) * 4, 0, SC1);
        o1 = __builtin_amdgcn_raw_buffer_load_b32(r_ws, o_grp + r8 * OREC + (8 * s + c8) * 4, 256, SC1);
        if (__all((ml[1] == T) & (ml[3] == T) & ((o0 & 0xffu) == want) & ((o1 & 0xffu) == want)) || spin > SPIN_CAP) break;
        __builtin_amdgcn_s_sleep(8); }
      const float mv = __uint_as_float(ml[0]), M = max_hi3(mv), e = __builtin_amdgcn_exp2f(mv - M);
      const float gl = g / sum_hi3(__uint_as_float(ml[2]) * e), fs = gl * e;
      if (lane == 0) { lds[L_MISC] = gl; lds[L_MISC + 1] = __uint_as_float(T); lds[L_MISC + 2] = M; }
      lds_signal(lds, C_S, lane);
      const float v0 = sum_hi3(fs * untag(o0)), v1 = sum_hi3(fs * untag(o1));
      const unsigned k0 = cvtpk(v0, dpp<XOR1>(v0)), k1 = cvtpk(v1, dpp<XOR1>(v1));
      float y[8];
#pragma unroll
      for (int k = 0; k < 8; ++k) y[k] = 0.f;
#pragma unroll
      for (int u = 0; u < 4; ++u) { mac_pair(y, wx[2 * u], wx[2 * u + 1], __builtin_amdgcn_readlane(k0, 2 * u)); mac_pair(y, wx[8 + 2 * u], wx[9 + 2 * u], __builtin_amdgcn_readlane(k1, 2 * u)); }
      if (lane < 56) st8(lds + L_Y + 8 * D + 8 * lane, y);
    }
    pre_addr();
    bar();
    pre_tail();
  }
  const unsigned T = __builtin_amdgcn_readfirstlane(__float_as_uint(Tv));
  const rsrc_t r_ws = mk(a.ws, WS_END);
  asm volatile("" : "+v"(base), "+v"(bias));
  bar();
  if (wv < 7) {
    float r[8];
#pragma unroll
    for (int k = 0; k < 8; ++k) r[k] = lds[l_b + k * D];
    const float v = (((r[0] + r[1]) + (r[2] + r[3])) + ((r[4] + r[5]) + (r[6] + r[7]))) + base;
    __builtin_amdgcn_raw_buffer_store_b32(__builtin_amdgcn_perm(__float_as_uint(v), T, tsel), r_ws, o_y, 0, SC1);
  }
  if (bid < NCONS) {
    if (wv > 0) {
      float v[4];
#define YLD(A) "buffer_load_dwordx4 v[" #A ":" #A "+3], %[o], %[rs], 0 offen sc1\n\tbuffer_load_dwordx4 v[" #A "+4:" #A "+7], %[o], %[rs], %[so] offen sc1\n\t"
#define YCK(A, L) "s_waitcnt vmcnt(4)\n\t" \
      "v_perm_b32 %[v0], v[" #A "+1], v[" #A "], %[sel]\n\tv_perm_b32 %[v1], v[" #A "+3], v[" #A "+2], %[sel]\n\t" \
      "v_perm_b32 %[v2], v[" #A "+5], v[" #A "+4], %[sel]\n\tv_perm_b32 %[v3], v[" #A "+7], v[" #A "+6], %[sel]\n\t" \
      "v_lshl_or_b32 %[v0], %[v1], 16, %[v0]\n\tv_lshl_or_b32 %[v2], %[v3], 16, %[v2]\n\t" \
      "v_xor_b32 %[v0], %[T], %[v0]\n\tv_xor_b32 %[v2], %[T], %[v2]\n\tv_or_b32 %[v0], %[v0], %[v2]\n\t" \
      "v_cmp_ne_u32 vcc, 0, %[v0]\n\ts_nop 4\n\ts_cbranch_vccz " L "\n\t"
#define YSUM(A) "v_and_b32 %[v0], %[m], v[" #A "]\n\tv_and_b32 %[v1], %[m], v[" #A "+1]\n\tv_and_b32 %[v2], %[m], v[" #A "+2]\n\tv_and_b32 %[v3], %[m], v[" #A "+3]\n\t" \
      "v_and_b32 v[" #A "+4], %[m], v[" #A "+4]\n\tv_and_b32 v[" #A "+5], %[m], v[" #A "+5]\n\tv_and_b32 v[" #A "+6], %[m], v[" #A "+6]\n\tv_and_b32 v[" #A "+7], %[m], v[" #A "+7]\n\t" \
      "v_add_f32 %[v0], %[v0], v[" #A "+4]\n\tv_add_f32 %[v1], %[v1], v[" #A "+5]\n\tv_add_f32 %[v2], %[v2], v[" #A "+6]\n\tv_add_f32 %[v3], %[v3], v[" #A "+7]\n\t"
      unsigned cnt = SPIN_CAP;
      asm volatile("s_sleep %[dl]\n\ts_nop 4\n\t" YLD(120) "s_sleep %[sl]\n\t" YLD(128) "s_sleep %[sl]\n\t" YLD(136)
        "1:\n\t" YCK(120, "2f") YLD(120) YCK(128, "3f") YLD(128) YCK(136, "4f") YLD(136)
        "s_sub_u32 %[cnt], %[cnt], 1\n\ts_cbranch_scc0 1b\n\ts_waitcnt vmcnt(0)\n\t"
        "2:\n\t" YSUM(120) "s_branch 5f\n\t"
        "3:\n\t" YSUM(128) "s_branch 5f\n\t"
        "4:\n\t" YSUM(136)
        "5:\n\ts_nop 1"
        : [v0] "=&v"(v[0]), [v1] "=&v"(v[1]), [v2] "=&v"(v[2]), [v3] "=&v"(v[3]), [cnt] "+s"(cnt)
        : [o] "v"(o_p), [rs] "s"(r_ws), [so] "s"(8 * D * 4), [T] "s"(T), [sel] "s"(0x0c0c0400u), [m] "s"(0xffffff00u), [sl] "i"(YSLEEP), [dl] "i"(YDELAY)
        : "vcc", "scc", "memory", "v120", "v121", "v122", "v123", "v124", "v125", "v126", "v127", "v128", "v129", "v130", "v131", "v132", "v133", "v134", "v135",
          "v136", "v137", "v138", "v139", "v140", "v141", "v142", "v143");
      v[0] = swap32_add(v[0], v[2]); v[1] = swap32_add(v[1], v[3]);
      v[0] = swap16_add(v[0], v[1]); v[0] += dpp<XOR8>(v[0]);
      if ((lane & 8) == 0) lds[o_r] = v[0];
    }
    bar();
    if (wv == 0) {
      float r[8];
#pragma unroll
      for (int k = 0; k < 8; ++k) r[k] = lds[L_RED + k * 32 + (lane & 31)];
      const float v = (((r[0] + r[1]) + (r[2] + r[3])) + ((r[4] + r[5]) + (r[6] + r[7]))) + bias;
      __builtin_amdgcn_raw_buffer_store_b32(cvtpk(v, dpp<XOR1>(v)), mk(a.out, 2 * D * 2), o_out, 0, 0);
    }
  }
  asm volatile(".Lcode_z:");
}

extern "C" void kernel_launch(void* const* d_in, const int* in_sizes, int n_in, void* d_out, int out_size, void* d_ws, size_t ws_size, hipStream_t stream) {
  Args a;
  a.x = (const unsigned short*)d_in[0]; a.kc = (const unsigned short*)d_in[1]; a.vc = (const unsigned short*)d_in[2];
  a.wq = (const unsigned short*)d_in[3]; a.wo = (const unsigned short*)d_in[4];
  a.w1 = (const unsigned short*)d_in[6]; a.b1 = (const unsigned short*)d_in[7]; a.w2 = (const unsigned short*)d_in[8]; a.b2 = (const unsigned short*)d_in[9];
  a.gw = (const unsigned short*)d_in[10];
  a.ws = (unsigned char*)d_ws; a.out = (unsigned short*)d_out;
  hipLaunchKernelGGL(fused, dim3(256), dim3(576), 0, stream, a);
}
```
